```python
import torch

def _softmax(x, axis=-1):
    return torch.softmax(x, dim=axis)
def _gelu(x):
    return torch.nn.functional.gelu(x, approximate="tanh")
def _silu(x):
    return torch.nn.functional.silu(x)
def _sigmoid(x):
    return torch.sigmoid(x)
def _layernorm(x, axis=-1, eps=1e-5):
    m = x.mean(dim=axis, keepdim=True)
    v = x.var(dim=axis, keepdim=True, unbiased=False)
    return (x - m) / torch.sqrt(v + eps)
def _scatter_add(tgt, idx, src, axis=0):
    return tgt.clone().index_add_(axis, idx, src)
def _segment_sum(src, seg, n):
    out = torch.zeros((n,) + src.shape[1:], dtype=src.dtype)
    return out.index_add_(0, seg, src)
def _pad(x, pw):
    flat = tuple(v for pair in reversed(pw) for v in pair)
    return torch.nn.functional.pad(x, flat)

kernel_name = 'standalone__softmax_full_bidir__decode'

def setup_inputs():

    _w = (lambda a: (lambda t: t.to(torch.bfloat16) if t.is_floating_point() else t)(torch.as_tensor(a)))
    B, H, Sq, Skv, Dh = 1, 32, 1, 4096, 128
    return {
        "q": _w((lambda *s: torch.randn(*s, dtype=torch.bfloat16) * 0.8)(B, H, Sq, Dh)),
        "k": _w((lambda *s: torch.randn(*s, dtype=torch.bfloat16) * 0.8)(B, H, Skv, Dh)),
        "v": _w((lambda *s: torch.randn(*s, dtype=torch.bfloat16) * 0.05)(B, H, Skv, Dh)),
    }

def reference(q, k, v):
    scale = q.shape[-1] ** -0.5
    scores = torch.einsum('bhqd,bhkd->bhqk', q, k) * scale
    attn = _softmax(scores, axis=-1)
    return torch.einsum('bhqk,bhkd->bhqd', attn, v)
```

```cpp
#define COMBINE 0
#include <hip/hip_runtime.h>
#include <hip/hip_bf16.h>
#include <cstdint>

#define NW 16
#define KPW 32
#define KV_NT 1

constexpr int H = 32, SKV = 4096, D = 128;
constexpr int NL = KPW / 4;
constexpr int KPB = NW * KPW;
constexpr int NSPLIT = SKV / KPB;
constexpr float C2 = 0.088388347648318440f * 1.4426950408889634f;

using u32x4 = __attribute__((ext_vector_type(4))) unsigned;
using u32x2 = __attribute__((ext_vector_type(2))) unsigned;
using f32x4 = __attribute__((ext_vector_type(4))) float;
using f32x2 = __attribute__((ext_vector_type(2))) float;
using bf16x2 = __attribute__((ext_vector_type(2))) __bf16;
using bf16x8 = __attribute__((ext_vector_type(8))) __bf16;
#define GAS __attribute__((address_space(1)))

__device__ __forceinline__ float dot2(unsigned k, unsigned q, float acc) {
  return __builtin_amdgcn_fdot2_f32_bf16(__builtin_bit_cast(bf16x2, k), __builtin_bit_cast(bf16x2, q), acc, false);
}
template <int CTRL> __device__ __forceinline__ float dpp(float x) {
  return __builtin_bit_cast(float, __builtin_amdgcn_mov_dpp(__builtin_bit_cast(int, x), CTRL, 0xf, 0xf, true));
}
constexpr int ROR8 = 0x128, ROR4 = 0x124, ROR2 = 0x122, ROR1 = 0x121;
__device__ __forceinline__ float row_allsum(float x) {
  x += dpp<ROR8>(x); x += dpp<ROR4>(x); x += dpp<ROR2>(x); x += dpp<ROR1>(x); return x;
}
__device__ __forceinline__ float xrow_max(float x) {
  auto s = __builtin_amdgcn_permlane16_swap(__float_as_uint(x), __float_as_uint(x), false, false);
  x = fmaxf(__uint_as_float(s[0]), __uint_as_float(s[1]));
  auto t = __builtin_amdgcn_permlane32_swap(__float_as_uint(x), __float_as_uint(x), false, false);
  return fmaxf(__uint_as_float(t[0]), __uint_as_float(t[1]));
}
__device__ __forceinline__ float xrow_sum(float x) {
  auto s = __builtin_amdgcn_permlane16_swap(__float_as_uint(x), __float_as_uint(x), false, false);
  x = __uint_as_float(s[0]) + __uint_as_float(s[1]);
  auto t = __builtin_amdgcn_permlane32_swap(__float_as_uint(x), __float_as_uint(x), false, false);
  return __uint_as_float(t[0]) + __uint_as_float(t[1]);
}
__device__ __forceinline__ unsigned pack_bf16(float lo, float hi) {
  bf16x2 r = {(__bf16)lo, (__bf16)hi};
  return __builtin_bit_cast(unsigned, r);
}
__device__ __forceinline__ u32x4 ldkv(const GAS u32x4* p) {
  return __builtin_nontemporal_load(p);
}

constexpr size_t WS_SLOT0 = 4096, SLOT_BYTES = 2048;
constexpr unsigned long long EPOCH_STEP = 0x9E3779B97F4A7C15ull;

__global__ void __launch_bounds__(NW * 64) attn_split(const unsigned short* __restrict__ q, const unsigned short* __restrict__ k,
                                                      const unsigned short* __restrict__ v, unsigned* __restrict__ out,
                                                      unsigned char* ws) {
  __shared__ __attribute__((aligned(16))) float lds[NW * (D + 4)];
  const int b = blockIdx.x, h = b / NSPLIT, sp = b % NSPLIT;
  const int tid = threadIdx.x, lane = tid & 63, c = lane & 15;
  const int wave = __builtin_amdgcn_readfirstlane(tid >> 6);
  const size_t base = ((size_t)b * KPB + (size_t)wave * KPW) * D * 2;
  const GAS u32x4* kp = (const GAS u32x4*)((const GAS unsigned char*)k + base) + lane;
  const GAS u32x4* vp = (const GAS u32x4*)((const GAS unsigned char*)v + base) + lane;
  const u32x4 qv = *((const GAS u32x4*)((const GAS unsigned char*)q + h * D * 2) + c);
  u32x4 K[NL], V[NL];
#pragma unroll
  for (int i = 0; i < NL; ++i) K[i] = ldkv(kp + i * 64);
#pragma unroll
  for (int i = 0; i < NL; ++i) V[i] = ldkv(vp + i * 64);
  __builtin_amdgcn_sched_barrier(0);
  unsigned msk[4];
#pragma unroll
  for (int j = 0; j < 4; ++j) msk[j] = (c >> 1) == j ? ((c & 1) ? 0xffff0000u : 0x0000ffffu) : 0u;

  float s[NL];
#pragma unroll
  for (int i = 0; i < NL; ++i) {
    float d = dot2(K[i][0], qv[0], 0.f);
    d = dot2(K[i][1], qv[1], d); d = dot2(K[i][2], qv[2], d); d = dot2(K[i][3], qv[3], d);
    s[i] = row_allsum(d);
  }
  float m = s[0];
#pragma unroll
  for (int i = 1; i < NL; ++i) m = fmaxf(m, s[i]);
  m = xrow_max(m) * C2;
  float l = 0.f;
  unsigned P[NL];
#pragma unroll
  for (int i = 0; i < NL; ++i) { const float p = __builtin_amdgcn_exp2f(fmaf(s[i], C2, -m)); l += p; P[i] = pack_bf16(p, p); }
  l = xrow_sum(l);
  f32x4 acc = {0.f, 0.f, 0.f, 0.f};
#pragma unroll
  for (int i = 0; i < NL; ++i) {
    const u32x4 bfrag = {P[i] & msk[0], P[i] & msk[1], P[i] & msk[2], P[i] & msk[3]};
    acc = __builtin_amdgcn_mfma_f32_16x16x32_bf16(__builtin_bit_cast(bf16x8, V[i]), __builtin_bit_cast(bf16x8, bfrag), acc, 0, 0, 0);
  }
  float* rec = lds + wave * (D + 4);
  if (c < 8) {
#pragma unroll
    for (int t = 0; t < 4; ++t) rec[4 + 32 * (lane >> 4) + 8 * t + c] = acc[t];
  }
  if (lane == 0) { rec[0] = m; rec[1] = l; }
  __syncthreads();
  if (wave != 0) return;
  float M = lds[0];
#pragma unroll
  for (int w = 1; w < NW; ++w) M = fmaxf(M, lds[w * (D + 4)]);
  float L = 0.f, o0 = 0.f, o1 = 0.f;
#pragma unroll
  for (int w = 0; w < NW; ++w) {
    const float* r = lds + w * (D + 4);
    const float wt = __builtin_amdgcn_exp2f(r[0] - M);
    const f32x2 a = *(const f32x2*)(r + 4 + 2 * lane);
    L = fmaf(wt, r[1], L); o0 = fmaf(wt, a[0], o0); o1 = fmaf(wt, a[1], o1);
  }
  float* slot = (float*)(ws + WS_SLOT0 + (size_t)b * SLOT_BYTES);
  *(f32x2*)(slot + 2 * lane) = f32x2{o0, o1};
  if (lane == 0) *(f32x2*)(slot + D) = f32x2{M, L};
}

__global__ void __launch_bounds__(64) attn_combine(const unsigned char* __restrict__ ws, unsigned* __restrict__ out) {
  const int h = blockIdx.x, lane = threadIdx.x;
  const float* s0 = (const float*)(ws + WS_SLOT0 + (size_t)h * NSPLIT * SLOT_BYTES);
  f32x2 a[NSPLIT], ml[NSPLIT];
#pragma unroll
  for (int j = 0; j < NSPLIT; ++j) { a[j] = *(const f32x2*)(s0 + j * (SLOT_BYTES / 4) + 2 * lane); ml[j] = *(const f32x2*)(s0 + j * (SLOT_BYTES / 4) + D); }
  float G = ml[0][0];
#pragma unroll
  for (int j = 1; j < NSPLIT; ++j) G = fmaxf(G, ml[j][0]);
  float L = 0.f, o0 = 0.f, o1 = 0.f;
#pragma unroll
  for (int j = 0; j < NSPLIT; ++j) {
    const float wt = __builtin_amdgcn_exp2f(ml[j][0] - G);
    L = fmaf(wt, ml[j][1], L); o0 = fmaf(wt, a[j][0], o0); o1 = fmaf(wt, a[j][1], o1);
  }
  const float inv = 1.f / L;
  out[h * (D / 2) + lane] = pack_bf16(o0 * inv, o1 * inv);
}

extern "C" void kernel_launch(void* const* d_in, const int* in_sizes, int n_in, void* d_out, int out_size, void* d_ws, size_t ws_size,
                              hipStream_t stream) {
  attn_split<<<H * NSPLIT, NW * 64, 0, stream>>>((const unsigned short*)d_in[0], (const unsigned short*)d_in[1], (const unsigned short*)d_in[2],
                                                 (unsigned*)d_out, (unsigned char*)d_ws);
  attn_combine<<<H, 64, 0, stream>>>((const unsigned char*)d_ws, (unsigned*)d_out);
}
```

```cpp
#define COMBINE 1
#include <hip/hip_runtime.h>
#include <hip/hip_bf16.h>
#include <cstdint>

#define NW 16
#define KPW 32
#define KV_NT 1

constexpr int H = 32, SKV = 4096, D = 128;
constexpr int NL = KPW / 4;
constexpr int KPB = NW * KPW;
constexpr int NSPLIT = SKV / KPB;
constexpr float C2 = 0.088388347648318440f * 1.4426950408889634f;

using u32x4 = __attribute__((ext_vector_type(4))) unsigned;
using u32x2 = __attribute__((ext_vector_type(2))) unsigned;
using f32x4 = __attribute__((ext_vector_type(4))) float;
using f32x2 = __attribute__((ext_vector_type(2))) float;
using bf16x2 = __attribute__((ext_vector_type(2))) __bf16;
using bf16x8 = __attribute__((ext_vector_type(8))) __bf16;
#define GAS __attribute__((address_space(1)))

__device__ __forceinline__ float dot2(unsigned k, unsigned q, float acc) {
  return __builtin_amdgcn_fdot2_f32_bf16(__builtin_bit_cast(bf16x2, k), __builtin_bit_cast(bf16x2, q), acc, false);
}
template <int CTRL> __device__ __forceinline__ float dpp(float x) {
  return __builtin_bit_cast(float, __builtin_amdgcn_mov_dpp(__builtin_bit_cast(int, x), CTRL, 0xf, 0xf, true));
}
constexpr int ROR8 = 0x128, ROR4 = 0x124, ROR2 = 0x122, ROR1 = 0x121;
__device__ __forceinline__ float row_allsum(float x) {
  x += dpp<ROR8>(x); x += dpp<ROR4>(x); x += dpp<ROR2>(x); x += dpp<ROR1>(x); return x;
}
__device__ __forceinline__ float xrow_max(float x) {
  auto s = __builtin_amdgcn_permlane16_swap(__float_as_uint(x), __float_as_uint(x), false, false);
  x = fmaxf(__uint_as_float(s[0]), __uint_as_float(s[1]));
  auto t = __builtin_amdgcn_permlane32_swap(__float_as_uint(x), __float_as_uint(x), false, false);
  return fmaxf(__uint_as_float(t[0]), __uint_as_float(t[1]));
}
__device__ __forceinline__ float xrow_sum(float x) {
  auto s = __builtin_amdgcn_permlane16_swap(__float_as_uint(x), __float_as_uint(x), false, false);
  x = __uint_as_float(s[0]) + __uint_as_float(s[1]);
  auto t = __builtin_amdgcn_permlane32_swap(__float_as_uint(x), __float_as_uint(x), false, false);
  return __uint_as_float(t[0]) + __uint_as_float(t[1]);
}
__device__ __forceinline__ unsigned pack_bf16(float lo, float hi) {
  bf16x2 r = {(__bf16)lo, (__bf16)hi};
  return __builtin_bit_cast(unsigned, r);
}
__device__ __forceinline__ u32x4 ldkv(const GAS u32x4* p) {
  return __builtin_nontemporal_load(p);
}

constexpr size_t WS_SLOT0 = 4096, SLOT_BYTES = 2048;
constexpr unsigned long long EPOCH_STEP = 0x9E3779B97F4A7C15ull;

__global__ void __launch_bounds__(NW * 64) attn_split(const unsigned short* __restrict__ q, const unsigned short* __restrict__ k,
                                                      const unsigned short* __restrict__ v, unsigned* __restrict__ out,
                                                      unsigned char* ws) {
  __shared__ __attribute__((aligned(16))) float lds[NW * (D + 4)];
  const int b = blockIdx.x, h = b / NSPLIT, sp = b % NSPLIT;
  const int tid = threadIdx.x, lane = tid & 63, c = lane & 15;
  const int wave = __builtin_amdgcn_readfirstlane(tid >> 6);
  const size_t base = ((size_t)b * KPB + (size_t)wave * KPW) * D * 2;
  const GAS u32x4* kp = (const GAS u32x4*)((const GAS unsigned char*)k + base) + lane;
  const GAS u32x4* vp = (const GAS u32x4*)((const GAS unsigned char*)v + base) + lane;
  const u32x4 qv = *((const GAS u32x4*)((const GAS unsigned char*)q + h * D * 2) + c);
  const unsigned long long tag = __hip_atomic_load((GAS unsigned long long*)(ws + h * 128), __ATOMIC_RELAXED, __HIP_MEMORY_SCOPE_AGENT) + EPOCH_STEP;
  u32x4 K[NL], V[NL];
#pragma unroll
  for (int i = 0; i < NL; ++i) K[i] = ldkv(kp + i * 64);
#pragma unroll
  for (int i = 0; i < NL; ++i) V[i] = ldkv(vp + i * 64);
  __builtin_amdgcn_sched_barrier(0);
  unsigned msk[4];
#pragma unroll
  for (int j = 0; j < 4; ++j) msk[j] = (c >> 1) == j ? ((c & 1) ? 0xffff0000u : 0x0000ffffu) : 0u;

  float s[NL];
#pragma unroll
  for (int i = 0; i < NL; ++i) {
    float d = dot2(K[i][0], qv[0], 0.f);
    d = dot2(K[i][1], qv[1], d); d = dot2(K[i][2], qv[2], d); d = dot2(K[i][3], qv[3], d);
    s[i] = row_allsum(d);
  }
  float m = s[0];
#pragma unroll
  for (int i = 1; i < NL; ++i) m = fmaxf(m, s[i]);
  m = xrow_max(m) * C2;
  float l = 0.f;
  unsigned P[NL];
#pragma unroll
  for (int i = 0; i < NL; ++i) { const float p = __builtin_amdgcn_exp2f(fmaf(s[i], C2, -m)); l += p; P[i] = pack_bf16(p, p); }
  l = xrow_sum(l);
  f32x4 acc = {0.f, 0.f, 0.f, 0.f};
#pragma unroll
  for (int i = 0; i < NL; ++i) {
    const u32x4 bfrag = {P[i] & msk[0], P[i] & msk[1], P[i] & msk[2], P[i] & msk[3]};
    acc = __builtin_amdgcn_mfma_f32_16x16x32_bf16(__builtin_bit_cast(bf16x8, V[i]), __builtin_bit_cast(bf16x8, bfrag), acc, 0, 0, 0);
  }
  float* rec = lds + wave * (D + 4);
  if (c < 8) {
#pragma unroll
    for (int t = 0; t < 4; ++t) rec[4 + 32 * (lane >> 4) + 8 * t + c] = acc[t];
  }
  if (lane == 0) { rec[0] = m; rec[1] = l; }
  __syncthreads();
  if (wave != 0) return;
  float M = lds[0];
#pragma unroll
  for (int w = 1; w < NW; ++w) M = fmaxf(M, lds[w * (D + 4)]);
  float L = 0.f, o0 = 0.f, o1 = 0.f;
#pragma unroll
  for (int w = 0; w < NW; ++w) {
    const float* r = lds + w * (D + 4);
    const float wt = __builtin_amdgcn_exp2f(r[0] - M);
    const f32x2 a = *(const f32x2*)(r + 4 + 2 * lane);
    L = fmaf(wt, r[1], L); o0 = fmaf(wt, a[0], o0); o1 = fmaf(wt, a[1], o1);
  }
  const unsigned tagA = (unsigned)tag, tagB = (unsigned)(tag >> 32);
  auto rsrc = __builtin_amdgcn_make_buffer_rsrc((void*)(ws + WS_SLOT0 + (size_t)h * NSPLIT * SLOT_BYTES), 0, NSPLIT * SLOT_BYTES, 0x00020000);
  if (sp != NSPLIT - 1) {
    __builtin_amdgcn_raw_buffer_store_b128(u32x4{__float_as_uint(o0), tagA, __float_as_uint(o1), tagB}, rsrc, sp * SLOT_BYTES + lane * 16, 0, 16);
    if (lane == 0) __builtin_amdgcn_raw_buffer_store_b128(u32x4{__float_as_uint(M), tagA, __float_as_uint(L), tagB}, rsrc, sp * SLOT_BYTES + 1024, 0, 16);
    return;
  }
  u32x4 a[NSPLIT - 1], ml[NSPLIT - 1];
  for (unsigned spins = 0;; ++spins) {
    bool ok = true;
#pragma unroll
    for (int j = 0; j < NSPLIT - 1; ++j) {
      a[j] = __builtin_amdgcn_raw_buffer_load_b128(rsrc, j * SLOT_BYTES + lane * 16, 0, 16 | (1u << 31));
      ml[j] = __builtin_amdgcn_raw_buffer_load_b128(rsrc, j * SLOT_BYTES + 1024, 0, 16 | (1u << 31));
    }
#pragma unroll
    for (int j = 0; j < NSPLIT - 1; ++j) ok &= (a[j][1] == tagA) & (a[j][3] == tagB) & (ml[j][1] == tagA) & (ml[j][3] == tagB);
    if (__all(ok)) break;
    if (spins > (1u << 18)) { M = __builtin_nanf(""); break; }
    asm volatile("" ::: "memory");
  }
  float G = M;
#pragma unroll
  for (int j = 0; j < NSPLIT - 1; ++j) G = fmaxf(G, __uint_as_float(ml[j][0]));
  { const float wt = __builtin_amdgcn_exp2f(M - G); L *= wt; o0 *= wt; o1 *= wt; }
#pragma unroll
  for (int j = 0; j < NSPLIT - 1; ++j) {
    const float wt = __builtin_amdgcn_exp2f(__uint_as_float(ml[j][0]) - G);
    L = fmaf(wt, __uint_as_float(ml[j][2]), L); o0 = fmaf(wt, __uint_as_float(a[j][0]), o0); o1 = fmaf(wt, __uint_as_float(a[j][2]), o1);
  }
  const float inv = 1.f / L;
  out[h * (D / 2) + lane] = pack_bf16(o0 * inv, o1 * inv);
  if (lane == 0) __hip_atomic_store((GAS unsigned long long*)(ws + h * 128), tag, __ATOMIC_RELAXED, __HIP_MEMORY_SCOPE_AGENT);
}

extern "C" void kernel_launch(void* const* d_in, const int* in_sizes, int n_in, void* d_out, int out_size, void* d_ws, size_t ws_size,
                              hipStream_t stream) {
  attn_split<<<H * NSPLIT, NW * 64, 0, stream>>>((const unsigned short*)d_in[0], (const unsigned short*)d_in[1], (const unsigned short*)d_in[2],
                                                 (unsigned*)d_out, (unsigned char*)d_ws);
}
```

```cpp
#include <hip/hip_runtime.h>
#include <cstdint>

__global__ void __launch_bounds__(576) attn_decode(const void* q, const void* k, const void* v, void* out, void* ws) {
  __shared__ __attribute__((aligned(16))) float lds[1024 + 32];
  const unsigned lds_base = (unsigned)(uintptr_t)(__attribute__((address_space(3))) float*)lds;
  asm volatile(
    "s_load_dwordx8 s[8:15], %0, 0x0\n"
    "v_readfirstlane_b32 s18, %2\n"
    "s_and_b32 s21, %1, 7\n"
    "s_lshr_b32 s22, %1, 6\n"
    "s_lshl2_add_u32 s19, s21, s22\n"
    "v_and_b32 v1, 63, %2\n"
    "s_cmpk_ge_u32 s18, 512\n"
    "s_cbranch_scc1 .Lhelper%=\n"
    "s_and_b32 s21, %1, 56\n"
    "s_lshl_b32 s21, s21, 12\n"
    "s_and_b32 s22, %1, 64\n"
    "s_lshl_b32 s22, s22, 13\n"
    "s_or_b32 s21, s21, s22\n"
    "s_and_b32 s22, %1, 1\n"
    "s_lshl_b32 s22, s22, 18\n"
    "s_or_b32 s21, s21, s22\n"
    "s_lshl_b32 s22, s18, 6\n"
    "s_or_b32 s21, s21, s22\n"
    "s_lshl_b32 s22, s19, 20\n"
    "s_or_b32 s21, s21, s22\n"
    "v_lshl_or_b32 v2, v1, 4, s21\n"
    "s_waitcnt lgkmcnt(0)\n"
    "global_load_dwordx4 v[64:67], v2, s[10:11] nt\n"
    "v_xor_b32 v3, 0xc0000, v2\n"
    "global_load_dwordx4 v[68:71], v3, s[10:11] offset:1024 nt\n"
    "global_load_dwordx4 v[72:75], v2, s[10:11] offset:2048 nt\n"
    "global_load_dwordx4 v[76:79], v3, s[10:11] offset:3072 nt\n"
    "v_and_b32 v22, 15, v1\n"
    "s_lshl_b32 s24, s19, 8\n"
    "v_lshl_or_b32 v23, v22, 4, s24\n"
    "global_load_dwordx4 v[6:9], v23, s[8:9]\n"
    "s_setprio 2\n"
    "global_load_dwordx4 v[80:83], v3, s[10:11] nt\n"
    "global_load_dwordx4 v[84:87], v2, s[10:11] offset:1024 nt\n"
    "global_load_dwordx4 v[88:91], v3, s[10:11] offset:2048 nt\n"
    "global_load_dwordx4 v[92:95], v2, s[10:11] offset:3072 nt\n"
    "v_xor_b32 v4, 0x40000, v2\n"
    "v_xor_b32 v5, 0x80000, v2\n"
    "s_bfe_u32 s20, %1, 0x30003\n"
    "s_load_dwordx2 s[16:17], %0, 0x20\n"
    "v_and_b32 v18, 1, v22\n"
    "v_lshlrev_b32 v18, 4, v18\n"
    "v_mov_b32 v19, 0xffff\n"
    "v_lshlrev_b32 v18, v18, v19\n"
    "v_lshrrev_b32 v19, 1, v22\n"
    "v_cmp_eq_u32 vcc, 0, v19\n"
    "s_nop 1\n"
    "v_cndmask_b32 v10, 0, v18, vcc\n"
    "v_cmp_eq_u32 vcc, 1, v19\n"
    "s_nop 1\n"
    "v_cndmask_b32 v11, 0, v18, vcc\n"
    "v_cmp_eq_u32 vcc, 2, v19\n"
    "s_nop 1\n"
    "v_cndmask_b32 v12, 0, v18, vcc\n"
    "v_cmp_eq_u32 vcc, 3, v19\n"
    "s_nop 1\n"
    "v_cndmask_b32 v13, 0, v18, vcc\n"
    "s_lshl_b32 s24, s18, 3\n"
    "s_add_u32 s24, s24, %3\n"
    "v_lshrrev_b32 v28, 4, v1\n"
    "v_lshlrev_b32 v28, 4, v28\n"
    "v_lshl_add_u32 v28, v22, 5, v28\n"
    "v_add_u32 v28, s24, v28\n"
    "s_lshr_b32 s24, s18, 3\n"
    "s_add_u32 s24, s24, %3\n"
    "v_mov_b32 v29, s24\n"
    "v_mov_b32 v30, %3\n"
    "s_waitcnt lgkmcnt(0)\n"
    "s_barrier\n"
    "v_mov_b32 v18, 0\n"
    "v_mov_b32 v19, 0\n"
    "v_mov_b32 v20, 0\n"
    "v_mov_b32 v21, 0\n"
    "s_waitcnt vmcnt(4)\n"
    "v_dot2c_f32_bf16 v18, v64, v6\n"
    "v_dot2c_f32_bf16 v19, v68, v6\n"
    "v_dot2c_f32_bf16 v20, v72, v6\n"
    "v_dot2c_f32_bf16 v21, v76, v6\n"
    "v_dot2c_f32_bf16 v18, v65, v7\n"
    "v_dot2c_f32_bf16 v19, v69, v7\n"
    "v_dot2c_f32_bf16 v20, v73, v7\n"
    "v_dot2c_f32_bf16 v21, v77, v7\n"
    "v_dot2c_f32_bf16 v18, v66, v8\n"
    "v_dot2c_f32_bf16 v19, v70, v8\n"
    "v_dot2c_f32_bf16 v20, v74, v8\n"
    "v_dot2c_f32_bf16 v21, v78, v8\n"
    "v_dot2c_f32_bf16 v18, v67, v9\n"
    "v_dot2c_f32_bf16 v19, v71, v9\n"
    "v_dot2c_f32_bf16 v20, v75, v9\n"
    "v_dot2c_f32_bf16 v21, v79, v9\n"
    "global_load_dwordx4 v[64:67], v4, s[10:11] nt\n"
    "global_load_dwordx4 v[68:71], v5, s[10:11] offset:1024 nt\n"
    "global_load_dwordx4 v[72:75], v4, s[10:11] offset:2048 nt\n"
    "global_load_dwordx4 v[76:79], v5, s[10:11] offset:3072 nt\n"
    "v_add_f32_dpp v22, v18, v18 row_ror:8 row_mask:0xf bank_mask:0x3\n"
    "v_add_f32_dpp v22, v19, v19 row_ror:8 row_mask:0xf bank_mask:0xc\n"
    "v_add_f32_dpp v23, v20, v20 row_ror:8 row_mask:0xf bank_mask:0x3\n"
    "v_add_f32_dpp v23, v21, v21 row_ror:8 row_mask:0xf bank_mask:0xc\n"
    "v_add_f32_dpp v14, v22, v22 row_half_mirror row_mask:0xf bank_mask:0x5\n"
    "s_nop 0\n"
    "v_add_f32_dpp v14, v23, v23 row_half_mirror row_mask:0xf bank_mask:0xa\n"
    "s_nop 1\n"
    "v_add_f32_dpp v14, v14, v14 quad_perm:[2,3,0,1] row_mask:0xf bank_mask:0xf\n"
    "s_nop 1\n"
    "v_add_f32_dpp v14, v14, v14 quad_perm:[1,0,3,2] row_mask:0xf bank_mask:0xf\n"
    "v_mov_b32 v18, 0\n"
    "v_mov_b32 v19, 0\n"
    "v_mov_b32 v20, 0\n"
    "v_mov_b32 v21, 0\n"
    "s_waitcnt vmcnt(4)\n"
    "v_dot2c_f32_bf16 v18, v80, v6\n"
    "v_dot2c_f32_bf16 v19, v84, v6\n"
    "v_dot2c_f32_bf16 v20, v88, v6\n"
    "v_dot2c_f32_bf16 v21, v92, v6\n"
    "v_dot2c_f32_bf16 v18, v81, v7\n"
    "v_dot2c_f32_bf16 v19, v85, v7\n"
    "v_dot2c_f32_bf16 v20, v89, v7\n"
    "v_dot2c_f32_bf16 v21, v93, v7\n"
    "v_dot2c_f32_bf16 v18, v82, v8\n"
    "v_dot2c_f32_bf16 v19, v86, v8\n"
    "v_dot2c_f32_bf16 v20, v90, v8\n"
    "v_dot2c_f32_bf16 v21, v94, v8\n"
    "v_dot2c_f32_bf16 v18, v83, v9\n"
    "v_dot2c_f32_bf16 v19, v87, v9\n"
    "v_dot2c_f32_bf16 v20, v91, v9\n"
    "v_dot2c_f32_bf16 v21, v95, v9\n"
    "global_load_dwordx4 v[80:83], v5, s[10:11] nt\n"
    "global_load_dwordx4 v[84:87], v4, s[10:11] offset:1024 nt\n"
    "global_load_dwordx4 v[88:91], v5, s[10:11] offset:2048 nt\n"
    "global_load_dwordx4 v[92:95], v4, s[10:11] offset:3072 nt\n"
    "v_add_f32_dpp v22, v18, v18 row_ror:8 row_mask:0xf bank_mask:0x3\n"
    "v_add_f32_dpp v22, v19, v19 row_ror:8 row_mask:0xf bank_mask:0xc\n"
    "v_add_f32_dpp v23, v20, v20 row_ror:8 row_mask:0xf bank_mask:0x3\n"
    "v_add_f32_dpp v23, v21, v21 row_ror:8 row_mask:0xf bank_mask:0xc\n"
    "v_add_f32_dpp v15, v22, v22 row_half_mirror row_mask:0xf bank_mask:0x5\n"
    "s_nop 0\n"
    "v_add_f32_dpp v15, v23, v23 row_half_mirror row_mask:0xf bank_mask:0xa\n"
    "s_nop 1\n"
    "v_add_f32_dpp v15, v15, v15 quad_perm:[2,3,0,1] row_mask:0xf bank_mask:0xf\n"
    "s_nop 1\n"
    "v_add_f32_dpp v15, v15, v15 quad_perm:[1,0,3,2] row_mask:0xf bank_mask:0xf\n"
    "v_mov_b32 v18, 0\n"
    "v_mov_b32 v19, 0\n"
    "v_mov_b32 v20, 0\n"
    "v_mov_b32 v21, 0\n"
    "s_waitcnt vmcnt(4)\n"
    "v_dot2c_f32_bf16 v18, v64, v6\n"
    "v_dot2c_f32_bf16 v19, v68, v6\n"
    "v_dot2c_f32_bf16 v20, v72, v6\n"
    "v_dot2c_f32_bf16 v21, v76, v6\n"
    "v_dot2c_f32_bf16 v18, v65, v7\n"
    "v_dot2c_f32_bf16 v19, v69, v7\n"
    "v_dot2c_f32_bf16 v20, v73, v7\n"
    "v_dot2c_f32_bf16 v21, v77, v7\n"
    "v_dot2c_f32_bf16 v18, v66, v8\n"
    "v_dot2c_f32_bf16 v19, v70, v8\n"
    "v_dot2c_f32_bf16 v20, v74, v8\n"
    "v_dot2c_f32_bf16 v21, v78, v8\n"
    "v_dot2c_f32_bf16 v18, v67, v9\n"
    "v_dot2c_f32_bf16 v19, v71, v9\n"
    "v_dot2c_f32_bf16 v20, v75, v9\n"
    "v_dot2c_f32_bf16 v21, v79, v9\n"
    "s_setprio 1\n"
    "global_load_dwordx4 v[64:67], v2, s[12:13] nt\n"
    "global_load_dwordx4 v[68:71], v3, s[12:13] offset:1024 nt\n"
    "global_load_dwordx4 v[72:75], v2, s[12:13] offset:2048 nt\n"
    "global_load_dwordx4 v[76:79], v3, s[12:13] offset:3072 nt\n"
    "v_add_f32_dpp v22, v18, v18 row_ror:8 row_mask:0xf bank_mask:0x3\n"
    "v_add_f32_dpp v22, v19, v19 row_ror:8 row_mask:0xf bank_mask:0xc\n"
    "v_add_f32_dpp v23, v20, v20 row_ror:8 row_mask:0xf bank_mask:0x3\n"
    "v_add_f32_dpp v23, v21, v21 row_ror:8 row_mask:0xf bank_mask:0xc\n"
    "v_add_f32_dpp v16, v22, v22 row_half_mirror row_mask:0xf bank_mask:0x5\n"
    "s_nop 0\n"
    "v_add_f32_dpp v16, v23, v23 row_half_mirror row_mask:0xf bank_mask:0xa\n"
    "s_nop 1\n"
    "v_add_f32_dpp v16, v16, v16 quad_perm:[2,3,0,1] row_mask:0xf bank_mask:0xf\n"
    "s_nop 1\n"
    "v_add_f32_dpp v16, v16, v16 quad_perm:[1,0,3,2] row_mask:0xf bank_mask:0xf\n"
    "v_mov_b32 v18, 0\n"
    "v_mov_b32 v19, 0\n"
    "v_mov_b32 v20, 0\n"
    "v_mov_b32 v21, 0\n"
    "s_waitcnt vmcnt(4)\n"
    "v_dot2c_f32_bf16 v18, v80, v6\n"
    "v_dot2c_f32_bf16 v19, v84, v6\n"
    "v_dot2c_f32_bf16 v20, v88, v6\n"
    "v_dot2c_f32_bf16 v21, v92, v6\n"
    "v_dot2c_f32_bf16 v18, v81, v7\n"
    "v_dot2c_f32_bf16 v19, v85, v7\n"
    "v_dot2c_f32_bf16 v20, v89, v7\n"
    "v_dot2c_f32_bf16 v21, v93, v7\n"
    "v_dot2c_f32_bf16 v18, v82, v8\n"
    "v_dot2c_f32_bf16 v19, v86, v8\n"
    "v_dot2c_f32_bf16 v20, v90, v8\n"
    "v_dot2c_f32_bf16 v21, v94, v8\n"
    "v_dot2c_f32_bf16 v18, v83, v9\n"
    "v_dot2c_f32_bf16 v19, v87, v9\n"
    "v_dot2c_f32_bf16 v20, v91, v9\n"
    "v_dot2c_f32_bf16 v21, v95, v9\n"
    "global_load_dwordx4 v[80:83], v3, s[12:13] nt\n"
    "global_load_dwordx4 v[84:87], v2, s[12:13] offset:1024 nt\n"
    "global_load_dwordx4 v[88:91], v3, s[12:13] offset:2048 nt\n"
    "global_load_dwordx4 v[92:95], v2, s[12:13] offset:3072 nt\n"
    "v_add_f32_dpp v22, v18, v18 row_ror:8 row_mask:0xf bank_mask:0x3\n"
    "v_add_f32_dpp v22, v19, v19 row_ror:8 row_mask:0xf bank_mask:0xc\n"
    "v_add_f32_dpp v23, v20, v20 row_ror:8 row_mask:0xf bank_mask:0x3\n"
    "v_add_f32_dpp v23, v21, v21 row_ror:8 row_mask:0xf bank_mask:0xc\n"
    "v_add_f32_dpp v17, v22, v22 row_half_mirror row_mask:0xf bank_mask:0x5\n"
    "s_nop 0\n"
    "v_add_f32_dpp v17, v23, v23 row_half_mirror row_mask:0xf bank_mask:0xa\n"
    "s_nop 1\n"
    "v_add_f32_dpp v17, v17, v17 quad_perm:[2,3,0,1] row_mask:0xf bank_mask:0xf\n"
    "s_nop 1\n"
    "v_add_f32_dpp v17, v17, v17 quad_perm:[1,0,3,2] row_mask:0xf bank_mask:0xf\n"
    "v_mul_f32 v14, 0x3e0293ee, v14\n"
    "v_mul_f32 v15, 0x3e0293ee, v15\n"
    "v_mul_f32 v16, 0x3e0293ee, v16\n"
    "v_mul_f32 v17, 0x3e0293ee, v17\n"
    "v_max3_f32 v22, v14, v15, v16\n"
    "v_max_f32 v22, v22, v17\n"
    "v_max_f32 v22, 0xff7fffff, v22\n"
    "s_nop 1\n"
    "v_max_f32_dpp v22, v22, v22 row_ror:4 row_mask:0xf bank_mask:0xf\n"
    "s_nop 1\n"
    "v_max_f32_dpp v22, v22, v22 row_ror:8 row_mask:0xf bank_mask:0xf\n"
    "s_nop 1\n"
    "v_max_f32_dpp v22, v22, v22 row_bcast:15 row_mask:0xa bank_mask:0xf\n"
    "s_nop 1\n"
    "v_max_f32_dpp v22, v22, v22 row_bcast:31 row_mask:0xc bank_mask:0xf\n"
    "s_nop 0\n"
    "v_readlane_b32 s24, v22, 63\n"
    "s_nop 1\n"
    "v_subrev_f32 v14, s24, v14\n"
    "v_subrev_f32 v15, s24, v15\n"
    "v_subrev_f32 v16, s24, v16\n"
    "v_subrev_f32 v17, s24, v17\n"
    "v_exp_f32 v14, v14\n"
    "v_exp_f32 v15, v15\n"
    "v_exp_f32 v16, v16\n"
    "v_exp_f32 v17, v17\n"
    "v_mov_b32 v31, 1\n"
    "v_add_f32 v23, v14, v15\n"
    "v_add_f32 v18, v16, v17\n"
    "v_add_f32 v23, v23, v18\n"
    "v_cvt_pk_bf16_f32 v14, v14, v14\n"
    "v_cvt_pk_bf16_f32 v15, v15, v15\n"
    "v_cvt_pk_bf16_f32 v16, v16, v16\n"
    "v_cvt_pk_bf16_f32 v17, v17, v17\n"
    "s_nop 1\n"
    "v_add_f32_dpp v23, v23, v23 row_ror:4 row_mask:0xf bank_mask:0xf\n"
    "s_nop 1\n"
    "v_add_f32_dpp v23, v23, v23 row_ror:8 row_mask:0xf bank_mask:0xf\n"
    "s_nop 1\n"
    "v_add_f32_dpp v23, v23, v23 row_bcast:15 row_mask:0xa bank_mask:0xf\n"
    "s_nop 1\n"
    "v_add_f32_dpp v23, v23, v23 row_bcast:31 row_mask:0xc bank_mask:0xf\n"
    "s_mov_b32 exec_lo, 0\n"
    "s_brev_b32 exec_hi, 1\n"
    "ds_write_b64 v29, v[22:23] offset:4096\n"
    "ds_add_u32 v30, v31 offset:4160\n"
    "s_mov_b64 exec, -1\n"
    "v_and_b32_dpp v32, v14, v10 row_newbcast:0 row_mask:0xf bank_mask:0xf\n"
    "v_and_b32_dpp v33, v14, v11 row_newbcast:0 row_mask:0xf bank_mask:0xf\n"
    "v_and_b32_dpp v34, v14, v12 row_newbcast:0 row_mask:0xf bank_mask:0xf\n"
    "v_and_b32_dpp v35, v14, v13 row_newbcast:0 row_mask:0xf bank_mask:0xf\n"
    "v_and_b32_dpp v36, v14, v10 row_newbcast:8 row_mask:0xf bank_mask:0xf\n"
    "v_and_b32_dpp v37, v14, v11 row_newbcast:8 row_mask:0xf bank_mask:0xf\n"
    "v_and_b32_dpp v38, v14, v12 row_newbcast:8 row_mask:0xf bank_mask:0xf\n"
    "v_and_b32_dpp v39, v14, v13 row_newbcast:8 row_mask:0xf bank_mask:0xf\n"
    "v_and_b32_dpp v40, v14, v10 row_newbcast:4 row_mask:0xf bank_mask:0xf\n"
    "v_and_b32_dpp v41, v14, v11 row_newbcast:4 row_mask:0xf bank_mask:0xf\n"
    "v_and_b32_dpp v42, v14, v12 row_newbcast:4 row_mask:0xf bank_mask:0xf\n"
    "v_and_b32_dpp v43, v14, v13 row_newbcast:4 row_mask:0xf bank_mask:0xf\n"
    "v_and_b32_dpp v44, v14, v10 row_newbcast:12 row_mask:0xf bank_mask:0xf\n"
    "v_and_b32_dpp v45, v14, v11 row_newbcast:12 row_mask:0xf bank_mask:0xf\n"
    "v_and_b32_dpp v46, v14, v12 row_newbcast:12 row_mask:0xf bank_mask:0xf\n"
    "v_and_b32_dpp v47, v14, v13 row_newbcast:12 row_mask:0xf bank_mask:0xf\n"
    "v_and_b32_dpp v48, v15, v10 row_newbcast:0 row_mask:0xf bank_mask:0xf\n"
    "v_and_b32_dpp v49, v15, v11 row_newbcast:0 row_mask:0xf bank_mask:0xf\n"
    "v_and_b32_dpp v50, v15, v12 row_newbcast:0 row_mask:0xf bank_mask:0xf\n"
    "v_and_b32_dpp v51, v15, v13 row_newbcast:0 row_mask:0xf bank_mask:0xf\n"
    "v_and_b32_dpp v52, v15, v10 row_newbcast:8 row_mask:0xf bank_mask:0xf\n"
    "v_and_b32_dpp v53, v15, v11 row_newbcast:8 row_mask:0xf bank_mask:0xf\n"
    "v_and_b32_dpp v54, v15, v12 row_newbcast:8 row_mask:0xf bank_mask:0xf\n"
    "v_and_b32_dpp v55, v15, v13 row_newbcast:8 row_mask:0xf bank_mask:0xf\n"
    "v_and_b32_dpp v56, v15, v10 row_newbcast:4 row_mask:0xf bank_mask:0xf\n"
    "v_and_b32_dpp v57, v15, v11 row_newbcast:4 row_mask:0xf bank_mask:0xf\n"
    "v_and_b32_dpp v58, v15, v12 row_newbcast:4 row_mask:0xf bank_mask:0xf\n"
    "v_and_b32_dpp v59, v15, v13 row_newbcast:4 row_mask:0xf bank_mask:0xf\n"
    "v_and_b32_dpp v60, v15, v10 row_newbcast:12 row_mask:0xf bank_mask:0xf\n"
    "v_and_b32_dpp v61, v15, v11 row_newbcast:12 row_mask:0xf bank_mask:0xf\n"
    "v_and_b32_dpp v62, v15, v12 row_newbcast:12 row_mask:0xf bank_mask:0xf\n"
    "v_and_b32_dpp v63, v15, v13 row_newbcast:12 row_mask:0xf bank_mask:0xf\n"
    "s_waitcnt vmcnt(7)\n"
    "v_mfma_f32_16x16x32_bf16 v[24:27], v[32:35], v[64:67], 0\n"
    "s_waitcnt vmcnt(6)\n"
    "v_mfma_f32_16x16x32_bf16 v[24:27], v[36:39], v[68:71], v[24:27]\n"
    "s_waitcnt vmcnt(5)\n"
    "v_mfma_f32_16x16x32_bf16 v[24:27], v[40:43], v[72:75], v[24:27]\n"
    "s_waitcnt vmcnt(4)\n"
    "v_mfma_f32_16x16x32_bf16 v[24:27], v[44:47], v[76:79], v[24:27]\n"
    "s_setprio 0\n"
    "global_load_dwordx4 v[64:67], v4, s[12:13] nt\n"
    "global_load_dwordx4 v[68:71], v5, s[12:13] offset:1024 nt\n"
    "global_load_dwordx4 v[72:75], v4, s[12:13] offset:2048 nt\n"
    "global_load_dwordx4 v[76:79], v5, s[12:13] offset:3072 nt\n"
    "v_and_b32_dpp v32, v16, v10 row_newbcast:0 row_mask:0xf bank_mask:0xf\n"
    "v_and_b32_dpp v33, v16, v11 row_newbcast:0 row_mask:0xf bank_mask:0xf\n"
    "v_and_b32_dpp v34, v16, v12 row_newbcast:0 row_mask:0xf bank_mask:0xf\n"
    "v_and_b32_dpp v35, v16, v13 row_newbcast:0 row_mask:0xf bank_mask:0xf\n"
    "v_and_b32_dpp v36, v16, v10 row_newbcast:8 row_mask:0xf bank_mask:0xf\n"
    "v_and_b32_dpp v37, v16, v11 row_newbcast:8 row_mask:0xf bank_mask:0xf\n"
    "v_and_b32_dpp v38, v16, v12 row_newbcast:8 row_mask:0xf bank_mask:0xf\n"
    "v_and_b32_dpp v39, v16, v13 row_newbcast:8 row_mask:0xf bank_mask:0xf\n"
    "v_and_b32_dpp v40, v16, v10 row_newbcast:4 row_mask:0xf bank_mask:0xf\n"
    "v_and_b32_dpp v41, v16, v11 row_newbcast:4 row_mask:0xf bank_mask:0xf\n"
    "v_and_b32_dpp v42, v16, v12 row_newbcast:4 row_mask:0xf bank_mask:0xf\n"
    "v_and_b32_dpp v43, v16, v13 row_newbcast:4 row_mask:0xf bank_mask:0xf\n"
    "v_and_b32_dpp v44, v16, v10 row_newbcast:12 row_mask:0xf bank_mask:0xf\n"
    "v_and_b32_dpp v45, v16, v11 row_newbcast:12 row_mask:0xf bank_mask:0xf\n"
    "v_and_b32_dpp v46, v16, v12 row_newbcast:12 row_mask:0xf bank_mask:0xf\n"
    "v_and_b32_dpp v47, v16, v13 row_newbcast:12 row_mask:0xf bank_mask:0xf\n"
    "s_waitcnt vmcnt(7)\n"
    "v_mfma_f32_16x16x32_bf16 v[24:27], v[48:51], v[80:83], v[24:27]\n"
    "s_waitcnt vmcnt(6)\n"
    "v_mfma_f32_16x16x32_bf16 v[24:27], v[52:55], v[84:87], v[24:27]\n"
    "s_waitcnt vmcnt(5)\n"
    "v_mfma_f32_16x16x32_bf16 v[24:27], v[56:59], v[88:91], v[24:27]\n"
    "s_waitcnt vmcnt(4)\n"
    "v_mfma_f32_16x16x32_bf16 v[24:27], v[60:63], v[92:95], v[24:27]\n"
    "global_load_dwordx4 v[80:83], v5, s[12:13] nt\n"
    "global_load_dwordx4 v[84:87], v4, s[12:13] offset:1024 nt\n"
    "global_load_dwordx4 v[88:91], v5, s[12:13] offset:2048 nt\n"
    "global_load_dwordx4 v[92:95], v4, s[12:13] offset:3072 nt\n"
    "s_mov_b64 exec, 1\n"
    "v_mov_b32 v31, 0x100\n"
    "ds_add_u32 v30, v31 offset:4160\n"
    "s_mov_b64 exec, -1\n"
    "v_and_b32_dpp v48, v17, v10 row_newbcast:0 row_mask:0xf bank_mask:0xf\n"
    "v_and_b32_dpp v49, v17, v11 row_newbcast:0 row_mask:0xf bank_mask:0xf\n"
    "v_and_b32_dpp v50, v17, v12 row_newbcast:0 row_mask:0xf bank_mask:0xf\n"
    "v_and_b32_dpp v51, v17, v13 row_newbcast:0 row_mask:0xf bank_mask:0xf\n"
    "v_and_b32_dpp v52, v17, v10 row_newbcast:8 row_mask:0xf bank_mask:0xf\n"
    "v_and_b32_dpp v53, v17, v11 row_newbcast:8 row_mask:0xf bank_mask:0xf\n"
    "v_and_b32_dpp v54, v17, v12 row_newbcast:8 row_mask:0xf bank_mask:0xf\n"
    "v_and_b32_dpp v55, v17, v13 row_newbcast:8 row_mask:0xf bank_mask:0xf\n"
    "v_and_b32_dpp v56, v17, v10 row_newbcast:4 row_mask:0xf bank_mask:0xf\n"
    "v_and_b32_dpp v57, v17, v11 row_newbcast:4 row_mask:0xf bank_mask:0xf\n"
    "v_and_b32_dpp v58, v17, v12 row_newbcast:4 row_mask:0xf bank_mask:0xf\n"
    "v_and_b32_dpp v59, v17, v13 row_newbcast:4 row_mask:0xf bank_mask:0xf\n"
    "v_and_b32_dpp v60, v17, v10 row_newbcast:12 row_mask:0xf bank_mask:0xf\n"
    "v_and_b32_dpp v61, v17, v11 row_newbcast:12 row_mask:0xf bank_mask:0xf\n"
    "v_and_b32_dpp v62, v17, v12 row_newbcast:12 row_mask:0xf bank_mask:0xf\n"
    "v_and_b32_dpp v63, v17, v13 row_newbcast:12 row_mask:0xf bank_mask:0xf\n"
    "v_lshrrev_b32 v7, 5, v1\n"
    "v_and_b32 v8, 31, v1\n"
    "v_lshlrev_b32 v8, 4, v8\n"
    "v_lshl_add_u32 v6, v7, 11, v8\n"
    "v_add_u32 v6, %3, v6\n"
    "s_lshl_b32 s24, s19, 12\n"
    "s_lshl_b32 s21, s20, 9\n"
    "s_add_u32 s24, s24, s21\n"
    "s_add_u32 s24, s24, 8192\n"
    "v_lshl_add_u32 v8, v1, 3, s24\n"
    "v_lshlrev_b32 v7, 4, v7\n"
    "v_add_u32 v7, %3, v7\n"
    "s_waitcnt vmcnt(7)\n"
    "v_mfma_f32_16x16x32_bf16 v[24:27], v[32:35], v[64:67], v[24:27]\n"
    "s_waitcnt vmcnt(6)\n"
    "v_mfma_f32_16x16x32_bf16 v[24:27], v[36:39], v[68:71], v[24:27]\n"
    "s_waitcnt vmcnt(5)\n"
    "v_mfma_f32_16x16x32_bf16 v[24:27], v[40:43], v[72:75], v[24:27]\n"
    "s_waitcnt vmcnt(4)\n"
    "v_mfma_f32_16x16x32_bf16 v[24:27], v[44:47], v[76:79], v[24:27]\n"
    "v_mov_b32 v31, 0x10000\n"
    "s_waitcnt vmcnt(3)\n"
    "v_mfma_f32_16x16x32_bf16 v[24:27], v[48:51], v[80:83], v[24:27]\n"
    "s_waitcnt vmcnt(2)\n"
    "v_mfma_f32_16x16x32_bf16 v[24:27], v[52:55], v[84:87], v[24:27]\n"
    "s_waitcnt vmcnt(1)\n"
    "v_mfma_f32_16x16x32_bf16 v[24:27], v[56:59], v[88:91], v[24:27]\n"
    "s_waitcnt vmcnt(0)\n"
    "v_mfma_f32_16x16x32_bf16 v[24:27], v[60:63], v[92:95], v[24:27]\n"
    "s_mov_b32 exec_hi, 0\n"
    "s_nop 6\n"
    "ds_write_b128 v28, v[24:27]\n"
    "s_mov_b64 exec, 1\n"
    "ds_add_rtn_u32 v9, v30, v31 offset:4160\n"
    "s_mov_b64 exec, -1\n"
    "ds_read_b128 v[20:23], v7 offset:4176\n"
    "ds_read_b128 v[32:35], v6\n"
    "ds_read_b128 v[36:39], v6 offset:512\n"
    "ds_read_b128 v[40:43], v6 offset:1024\n"
    "ds_read_b128 v[44:47], v6 offset:1536\n"
    "s_waitcnt lgkmcnt(3)\n"
    "v_pk_mul_f32 v[10:11], v[32:33], v[20:21] op_sel_hi:[1,0]\n"
    "v_pk_mul_f32 v[12:13], v[34:35], v[20:21] op_sel_hi:[1,0]\n"
    "v_readfirstlane_b32 s31, v9\n"
    "s_waitcnt lgkmcnt(2)\n"
    "v_pk_fma_f32 v[10:11], v[36:37], v[20:21], v[10:11] op_sel:[0,1,0] op_sel_hi:[1,1,1]\n"
    "v_pk_fma_f32 v[12:13], v[38:39], v[20:21], v[12:13] op_sel:[0,1,0] op_sel_hi:[1,1,1]\n"
    "s_waitcnt lgkmcnt(1)\n"
    "v_pk_fma_f32 v[10:11], v[40:41], v[22:23], v[10:11] op_sel_hi:[1,0,1]\n"
    "v_pk_fma_f32 v[12:13], v[42:43], v[22:23], v[12:13] op_sel_hi:[1,0,1]\n"
    "s_waitcnt lgkmcnt(0)\n"
    "v_pk_fma_f32 v[10:11], v[44:45], v[22:23], v[10:11] op_sel:[0,1,0] op_sel_hi:[1,1,1]\n"
    "v_pk_fma_f32 v[12:13], v[46:47], v[22:23], v[12:13] op_sel:[0,1,0] op_sel_hi:[1,1,1]\n"
    "s_bitcmp0_b32 s31, 19\n"
    "s_cbranch_scc1 .Lsend%=\n"
    "v_permlane32_swap_b32 v10, v12\n"
    "v_permlane32_swap_b32 v11, v13\n"
    "v_add_f32 v10, v10, v12\n"
    "v_add_f32 v11, v11, v13\n"
    "v_min_u32 v10, -2, v10\n"
    "v_min_u32 v11, -2, v11\n"
    "global_store_dwordx2 v8, v[10:11], s[16:17] sc1\n"
    ".Lsend%=:\n"
    "s_endpgm\n"
    ".Lhelper%=:\n"
    "v_mov_b32 v25, %3\n"
    "v_mov_b32 v26, 0\n"
    "ds_write_b32 v25, v26 offset:4160\n"
    "s_load_dwordx2 s[16:17], %0, 0x20\n"
    "s_bfe_u32 s20, %1, 0x30003\n"
    "s_lshl_b32 s28, s19, 7\n"
    "v_mov_b32 v30, s28\n"
    "s_waitcnt lgkmcnt(0)\n"
    "s_barrier\n"
    "s_setprio 3\n"
    "global_load_dwordx2 v[14:15], v30, s[16:17] sc1\n"
    "v_lshrrev_b32 v7, 5, v1\n"
    "v_and_b32 v8, 31, v1\n"
    "v_lshlrev_b32 v8, 4, v8\n"
    "v_lshl_add_u32 v6, v7, 11, v8\n"
    "v_add_u32 v6, %3, v6\n"
    "s_lshl_b32 s24, s19, 12\n"
    "s_lshl_b32 s21, s20, 9\n"
    "s_add_u32 s24, s24, s21\n"
    "s_add_u32 s24, s24, 8192\n"
    "v_lshl_add_u32 v8, v1, 3, s24\n"
    "v_lshlrev_b32 v7, 4, v7\n"
    "v_add_u32 v7, %3, v7\n"
    "v_mov_b32 v10, -1\n"
    "v_mov_b32 v11, -1\n"
    "global_store_dwordx2 v8, v[10:11], s[16:17] sc1\n"
    "v_and_b32 v24, 15, v1\n"
    "v_lshlrev_b32 v24, 4, v24\n"
    "v_lshrrev_b32 v12, 5, v1\n"
    "v_lshl_add_u32 v24, v12, 3, v24\n"
    "v_bfe_u32 v12, v1, 4, 1\n"
    "v_lshl_add_u32 v24, v12, 2, v24\n"
    "s_lshl_b32 s25, s19, 8\n"
    "v_add_u32 v24, s25, v24\n"
    "s_lshl_b32 s25, s19, 12\n"
    "s_add_u32 s25, s25, 8192\n"
    "v_lshl_add_u32 v2, v1, 4, s25\n"
    "v_and_b32 v13, 7, v1\n"
    "s_add_u32 s24, s28, 4096\n"
    "v_lshl_add_u32 v3, v13, 4, s24\n"
    "v_lshl_add_u32 v5, v13, 3, v25\n"
    "v_lshl_add_u32 v4, v1, 2, v25\n"
    "v_cmp_ne_u32 s[28:29], 7, v13\n"
    "s_mov_b32 s30, 0\n"
    "s_waitcnt vmcnt(0)\n"
    "v_readfirstlane_b32 s26, v14\n"
    "v_readfirstlane_b32 s27, v15\n"
    "s_nop 3\n"
    "s_add_u32 s26, s26, 0x7f4a7c15\n"
    "s_addc_u32 s27, s27, 0x9e3779b9\n"
    "v_mov_b32 v49, s26\n"
    "v_mov_b32 v51, s27\n"
    ".Lwml%=:\n"
    "s_sleep 4\n"
    "ds_read_b32 v26, v25 offset:4160\n"
    "s_waitcnt lgkmcnt(0)\n"
    "v_readfirstlane_b32 s31, v26\n"
    "s_nop 3\n"
    "s_bitcmp1_b32 s31, 3\n"
    "s_cbranch_scc0 .Lwml%=\n"
    "ds_read_b64 v[14:15], v5 offset:4096\n"
    "s_waitcnt lgkmcnt(0)\n"
    "v_mov_b32 v48, v14\n"
    "s_nop 1\n"
    "v_max_f32_dpp v48, v48, v48 row_ror:4 row_mask:0xf bank_mask:0xf\n"
    "s_nop 1\n"
    "v_max_f32_dpp v48, v48, v48 row_ror:2 row_mask:0xf bank_mask:0xf\n"
    "s_nop 1\n"
    "v_max_f32_dpp v48, v48, v48 row_ror:1 row_mask:0xf bank_mask:0xf\n"
    "v_sub_f32 v28, v14, v48\n"
    "v_exp_f32 v28, v28\n"
    "s_nop 0\n"
    "v_mul_f32 v50, v15, v28\n"
    "s_nop 1\n"
    "v_add_f32_dpp v50, v50, v50 row_ror:4 row_mask:0xf bank_mask:0xf\n"
    "s_nop 1\n"
    "v_add_f32_dpp v50, v50, v50 row_ror:2 row_mask:0xf bank_mask:0xf\n"
    "s_nop 1\n"
    "v_add_f32_dpp v50, v50, v50 row_ror:1 row_mask:0xf bank_mask:0xf\n"
    "s_lshl4_add_u32 s24, s20, s24\n"
    "v_mov_b32 v12, s24\n"
    "s_cmp_eq_u32 s20, 7\n"
    "s_cbranch_scc1 .Lnoml%=\n"
    "s_mov_b64 exec, 1\n"
    "global_store_dwordx4 v12, v[48:51], s[16:17] sc1\n"
    ".Lnoml%=:\n"
    "s_mov_b64 exec, 0xff\n"
    "ds_write_b32 v4, v28 offset:4176\n"
    "s_mov_b64 exec, 1\n"
    "v_mov_b32 v31, 0x10000\n"
    "ds_add_rtn_u32 v9, v25, v31 offset:4160\n"
    "s_mov_b64 exec, -1\n"
    "ds_read_b128 v[20:23], v7 offset:4176\n"
    "ds_read_b128 v[32:35], v6\n"
    "ds_read_b128 v[36:39], v6 offset:512\n"
    "ds_read_b128 v[40:43], v6 offset:1024\n"
    "ds_read_b128 v[44:47], v6 offset:1536\n"
    "s_waitcnt lgkmcnt(3)\n"
    "v_pk_mul_f32 v[10:11], v[32:33], v[20:21] op_sel_hi:[1,0]\n"
    "v_pk_mul_f32 v[12:13], v[34:35], v[20:21] op_sel_hi:[1,0]\n"
    "v_readfirstlane_b32 s31, v9\n"
    "s_waitcnt lgkmcnt(2)\n"
    "v_pk_fma_f32 v[10:11], v[36:37], v[20:21], v[10:11] op_sel:[0,1,0] op_sel_hi:[1,1,1]\n"
    "v_pk_fma_f32 v[12:13], v[38:39], v[20:21], v[12:13] op_sel:[0,1,0] op_sel_hi:[1,1,1]\n"
    "s_waitcnt lgkmcnt(1)\n"
    "v_pk_fma_f32 v[10:11], v[40:41], v[22:23], v[10:11] op_sel_hi:[1,0,1]\n"
    "v_pk_fma_f32 v[12:13], v[42:43], v[22:23], v[12:13] op_sel_hi:[1,0,1]\n"
    "s_waitcnt lgkmcnt(0)\n"
    "v_pk_fma_f32 v[10:11], v[44:45], v[22:23], v[10:11] op_sel:[0,1,0] op_sel_hi:[1,1,1]\n"
    "v_pk_fma_f32 v[12:13], v[46:47], v[22:23], v[12:13] op_sel:[0,1,0] op_sel_hi:[1,1,1]\n"
    "s_bitcmp0_b32 s31, 19\n"
    "s_cbranch_scc1 .Lhskip%=\n"
    "v_permlane32_swap_b32 v10, v12\n"
    "v_permlane32_swap_b32 v11, v13\n"
    "v_add_f32 v10, v10, v12\n"
    "v_add_f32 v11, v11, v13\n"
    "v_min_u32 v10, -2, v10\n"
    "v_min_u32 v11, -2, v11\n"
    "global_store_dwordx2 v8, v[10:11], s[16:17] sc1\n"
    ".Lhskip%=:\n"
    "s_cmp_eq_u32 s20, 7\n"
    "s_cbranch_scc1 .Lcons%=\n"
    "s_endpgm\n"
    ".Lcons%=:\n"
    "v_mov_b32 v12, s26\n"
    "v_mov_b32 v13, s27\n"
    ".Lciss%=:\n"
    "s_sleep 2\n"
    "ds_read_b32 v26, v25 offset:4160\n"
    "s_waitcnt lgkmcnt(0)\n"
    "v_readfirstlane_b32 s31, v26\n"
    "s_nop 3\n"
    "s_bitcmp1_b32 s31, 11\n"
    "s_cbranch_scc0 .Lciss%=\n"
    "s_branch .Lfirst%=\n"
    ".Lretry1%=:\n"
    "s_add_u32 s30, s30, 1\n"
    "s_cmp_gt_u32 s30, 262144\n"
    "s_cbranch_scc1 .Lfail%=\n"
    ".Lfirst%=:\n"
    "global_load_dwordx4 v[16:19], v3, s[16:17] sc1\n"
    "s_waitcnt vmcnt(0)\n"
    "v_xor_b32 v27, s26, v17\n"
    "v_bitop3_b32 v27, v19, s27, v27 bitop3:0xbe\n"
    "v_cmp_ne_u32 vcc, 0, v27\n"
    "s_and_b64 vcc, vcc, s[28:29]\n"
    "s_cbranch_vccnz .Lretry1%=\n"
    "s_mov_b64 exec, 1\n"
    "global_store_dwordx2 v30, v[12:13], s[16:17] sc1\n"
    "s_mov_b64 exec, -1\n"
    "global_load_dwordx4 v[64:67], v2, s[16:17] sc1\n"
    "global_load_dwordx4 v[68:71], v2, s[16:17] offset:1024 sc1\n"
    "global_load_dwordx4 v[72:75], v2, s[16:17] offset:2048 sc1\n"
    "global_load_dwordx4 v[76:79], v2, s[16:17] offset:3072 sc1\n"
    "global_load_dwordx4 v[80:83], v2, s[16:17] sc1\n"
    "global_load_dwordx4 v[84:87], v2, s[16:17] offset:1024 sc1\n"
    "global_load_dwordx4 v[88:91], v2, s[16:17] offset:2048 sc1\n"
    "global_load_dwordx4 v[92:95], v2, s[16:17] offset:3072 sc1\n"
    "v_cndmask_b32 v14, v48, v16, s[28:29]\n"
    "v_cndmask_b32 v15, v50, v18, s[28:29]\n"
    "v_mov_b32 v29, v14\n"
    "s_nop 1\n"
    "v_max_f32_dpp v29, v29, v29 row_ror:4 row_mask:0xf bank_mask:0xf\n"
    "s_nop 1\n"
    "v_max_f32_dpp v29, v29, v29 row_ror:2 row_mask:0xf bank_mask:0xf\n"
    "s_nop 1\n"
    "v_max_f32_dpp v29, v29, v29 row_ror:1 row_mask:0xf bank_mask:0xf\n"
    "v_sub_f32 v14, v14, v29\n"
    "v_exp_f32 v14, v14\n"
    "s_nop 0\n"
    "v_mul_f32 v15, v15, v14\n"
    "s_nop 1\n"
    "v_add_f32_dpp v15, v15, v15 row_ror:4 row_mask:0xf bank_mask:0xf\n"
    "s_nop 1\n"
    "v_add_f32_dpp v15, v15, v15 row_ror:2 row_mask:0xf bank_mask:0xf\n"
    "s_nop 1\n"
    "v_add_f32_dpp v15, v15, v15 row_ror:1 row_mask:0xf bank_mask:0xf\n"
    "v_rcp_f32 v15, v15\n"
    "s_nop 0\n"
    "v_mul_f32 v14, v14, v15\n"
    "s_nop 1\n"
    "v_mov_b32_dpp v56, v14 row_newbcast:0 row_mask:0x3 bank_mask:0xf\n"
    "v_mov_b32_dpp v56, v14 row_newbcast:1 row_mask:0xc bank_mask:0xf\n"
    "v_mov_b32_dpp v58, v14 row_newbcast:2 row_mask:0x3 bank_mask:0xf\n"
    "v_mov_b32_dpp v58, v14 row_newbcast:3 row_mask:0xc bank_mask:0xf\n"
    "v_mov_b32_dpp v60, v14 row_newbcast:4 row_mask:0x3 bank_mask:0xf\n"
    "v_mov_b32_dpp v60, v14 row_newbcast:5 row_mask:0xc bank_mask:0xf\n"
    "v_mov_b32_dpp v62, v14 row_newbcast:6 row_mask:0x3 bank_mask:0xf\n"
    "v_mov_b32_dpp v62, v14 row_newbcast:7 row_mask:0xc bank_mask:0xf\n"
    ".Lpoll%=:\n"
    "s_waitcnt vmcnt(7)\n"
    "v_max_u32 v27, v64, v65\n"
    "v_max3_u32 v27, v66, v67, v27\n"
    "v_pk_mul_f32 v[52:53], v[64:65], v[56:57] op_sel_hi:[1,0]\n"
    "v_pk_mul_f32 v[54:55], v[66:67], v[56:57] op_sel_hi:[1,0]\n"
    "s_waitcnt vmcnt(6)\n"
    "v_max3_u32 v27, v68, v69, v27\n"
    "v_max3_u32 v27, v70, v71, v27\n"
    "v_pk_fma_f32 v[52:53], v[68:69], v[58:59], v[52:53] op_sel_hi:[1,0,1]\n"
    "v_pk_fma_f32 v[54:55], v[70:71], v[58:59], v[54:55] op_sel_hi:[1,0,1]\n"
    "s_waitcnt vmcnt(5)\n"
    "v_max3_u32 v27, v72, v73, v27\n"
    "v_max3_u32 v27, v74, v75, v27\n"
    "v_pk_fma_f32 v[52:53], v[72:73], v[60:61], v[52:53] op_sel_hi:[1,0,1]\n"
    "v_pk_fma_f32 v[54:55], v[74:75], v[60:61], v[54:55] op_sel_hi:[1,0,1]\n"
    "s_waitcnt vmcnt(4)\n"
    "v_max3_u32 v27, v76, v77, v27\n"
    "v_max3_u32 v27, v78, v79, v27\n"
    "v_cmp_eq_u32 vcc, -1, v27\n"
    "v_pk_fma_f32 v[52:53], v[76:77], v[62:63], v[52:53] op_sel_hi:[1,0,1]\n"
    "v_pk_fma_f32 v[54:55], v[78:79], v[62:63], v[54:55] op_sel_hi:[1,0,1]\n"
    "s_cbranch_vccz .Lglob%=\n"
    "global_load_dwordx4 v[64:67], v2, s[16:17] sc1\n"
    "global_load_dwordx4 v[68:71], v2, s[16:17] offset:1024 sc1\n"
    "global_load_dwordx4 v[72:75], v2, s[16:17] offset:2048 sc1\n"
    "global_load_dwordx4 v[76:79], v2, s[16:17] offset:3072 sc1\n"
    "s_waitcnt vmcnt(7)\n"
    "v_max_u32 v27, v80, v81\n"
    "v_max3_u32 v27, v82, v83, v27\n"
    "v_pk_mul_f32 v[52:53], v[80:81], v[56:57] op_sel_hi:[1,0]\n"
    "v_pk_mul_f32 v[54:55], v[82:83], v[56:57] op_sel_hi:[1,0]\n"
    "s_waitcnt vmcnt(6)\n"
    "v_max3_u32 v27, v84, v85, v27\n"
    "v_max3_u32 v27, v86, v87, v27\n"
    "v_pk_fma_f32 v[52:53], v[84:85], v[58:59], v[52:53] op_sel_hi:[1,0,1]\n"
    "v_pk_fma_f32 v[54:55], v[86:87], v[58:59], v[54:55] op_sel_hi:[1,0,1]\n"
    "s_waitcnt vmcnt(5)\n"
    "v_max3_u32 v27, v88, v89, v27\n"
    "v_max3_u32 v27, v90, v91, v27\n"
    "v_pk_fma_f32 v[52:53], v[88:89], v[60:61], v[52:53] op_sel_hi:[1,0,1]\n"
    "v_pk_fma_f32 v[54:55], v[90:91], v[60:61], v[54:55] op_sel_hi:[1,0,1]\n"
    "s_waitcnt vmcnt(4)\n"
    "v_max3_u32 v27, v92, v93, v27\n"
    "v_max3_u32 v27, v94, v95, v27\n"
    "v_cmp_eq_u32 vcc, -1, v27\n"
    "v_pk_fma_f32 v[52:53], v[92:93], v[62:63], v[52:53] op_sel_hi:[1,0,1]\n"
    "v_pk_fma_f32 v[54:55], v[94:95], v[62:63], v[54:55] op_sel_hi:[1,0,1]\n"
    "s_cbranch_vccz .Lglob%=\n"
    "global_load_dwordx4 v[80:83], v2, s[16:17] sc1\n"
    "global_load_dwordx4 v[84:87], v2, s[16:17] offset:1024 sc1\n"
    "global_load_dwordx4 v[88:91], v2, s[16:17] offset:2048 sc1\n"
    "global_load_dwordx4 v[92:95], v2, s[16:17] offset:3072 sc1\n"
    "s_add_u32 s30, s30, 1\n"
    "s_cmp_le_u32 s30, 262144\n"
    "s_cbranch_scc1 .Lpoll%=\n"
    ".Lfail%=:\n"
    "s_waitcnt vmcnt(0)\n"
    "v_mov_b32 v52, 0x7fc00000\n"
    "v_mov_b32 v53, 0x7fc00000\n"
    "v_mov_b32 v54, 0x7fc00000\n"
    "v_mov_b32 v55, 0x7fc00000\n"
    "s_nop 0\n"
    ".Lglob%=:\n"
    "s_nop 0\n"
    "v_permlane32_swap_b32 v52, v54\n"
    "v_permlane32_swap_b32 v53, v55\n"
    "v_add_f32 v52, v52, v54\n"
    "v_add_f32 v53, v53, v55\n"
    "v_cvt_pk_bf16_f32 v52, v52, v53\n"
    "global_store_dword v24, v52, s[14:15]\n"
    "s_endpgm\n"
    :
    : "s"(__builtin_amdgcn_kernarg_segment_ptr()), "s"(__builtin_amdgcn_workgroup_id_x()), "v"(__builtin_amdgcn_workitem_id_x()),
      "s"(lds_base)
    :
      "v1", "v2", "v3", "v4", "v5", "v6", "v7", "v8", "v9", "v10", "v11", "v12", "v13", "v14", "v15", "v16",
      "v17", "v18", "v19", "v20", "v21", "v22", "v23", "v24", "v25", "v26", "v27", "v28", "v29", "v30", "v31", "v32",
      "v33", "v34", "v35", "v36", "v37", "v38", "v39", "v40", "v41", "v42", "v43", "v44", "v45", "v46", "v47", "v48",
      "v49", "v50", "v51", "v52", "v53", "v54", "v55", "v56", "v57", "v58", "v59", "v60", "v61", "v62", "v63", "v64",
      "v65", "v66", "v67", "v68", "v69", "v70", "v71", "v72", "v73", "v74", "v75", "v76", "v77", "v78", "v79", "v80",
      "v81", "v82", "v83", "v84", "v85", "v86", "v87", "v88", "v89", "v90", "v91", "v92", "v93", "v94", "v95", "v96",
      "v97", "v98", "v99", "s8", "s9", "s10", "s11", "s12", "s13", "s14", "s15", "s16", "s17", "s18", "s19", "s20",
      "s21", "s22", "s23", "s24", "s25", "s26", "s27", "s28", "s29", "s30", "s31", "vcc", "scc", "memory");
}

extern "C" void kernel_launch(void* const* d_in, const int* in_sizes, int n_in, void* d_out, int out_size, void* d_ws, size_t ws_size,
                              hipStream_t stream) {
  attn_decode<<<256, 576, 0, stream>>>(d_in[0], d_in[1], d_in[2], d_out, d_ws);
}
```
